```python
import torch

def _softmax(x, axis=-1):
    return torch.softmax(x, dim=axis)
def _gelu(x):
    return torch.nn.functional.gelu(x, approximate="tanh")
def _silu(x):
    return torch.nn.functional.silu(x)
def _sigmoid(x):
    return torch.sigmoid(x)
def _layernorm(x, axis=-1, eps=1e-5):
    m = x.mean(dim=axis, keepdim=True)
    v = x.var(dim=axis, keepdim=True, unbiased=False)
    return (x - m) / torch.sqrt(v + eps)
def _scatter_add(tgt, idx, src, axis=0):
    return tgt.clone().index_add_(axis, idx, src)
def _segment_sum(src, seg, n):
    out = torch.zeros((n,) + src.shape[1:], dtype=src.dtype)
    return out.index_add_(0, seg, src)
def _pad(x, pw):
    flat = tuple(v for pair in reversed(pw) for v in pair)
    return torch.nn.functional.pad(x, flat)

kernel_name = 'sequential_preLN__mha_block_cached__geglu_ffn__decode'

def setup_inputs():

    _w = (lambda a: (lambda t: t.to(torch.bfloat16) if t.is_floating_point() else t)(torch.as_tensor(a)))
    def _A_setup():
        B, Sq, D = 1, 1, 512
        H, Sk, Dh = 32, 16384, 128
        return {
            "x": _w((lambda *s: torch.randn(*s, dtype=torch.bfloat16) * 0.05)(B, Sq, D)),
            "k_cache": _w((lambda *s: torch.randn(*s, dtype=torch.bfloat16) * 3.2)(B, H, Sk, Dh)),
            "v_cache": _w((lambda *s: torch.randn(*s, dtype=torch.bfloat16) * 0.4)(B, H, Sk, Dh)),
            "wq": _w((lambda *s: torch.randn(*s, dtype=torch.bfloat16) * 3.2)(D, H * Dh) * (D ** -0.5)),
            "wo": _w((lambda *s: torch.randn(*s, dtype=torch.bfloat16) * 0.4)(H * Dh, D) * ((H * Dh) ** -0.5)),
        }
    _A_in = _A_setup()

    def _B_setup():
        B, S, D, F = 1, 1, 512, 1792
        return {
            "x": _w((lambda *s: torch.randn(*s, dtype=torch.bfloat16) * 0.05)(B, S, D)),
            "w_gate": _w((lambda *s: torch.randn(*s, dtype=torch.bfloat16) * 0.05)(D, F)),
            "w_up": _w((lambda *s: torch.randn(*s, dtype=torch.bfloat16) * 0.05)(D, F)),
            "w_down": _w((lambda *s: torch.randn(*s, dtype=torch.bfloat16) * 0.05)(F, D)),
        }
    _B_in = _B_setup()

    return {
        "A_x": _A_in["x"],
        "A_k_cache": _A_in["k_cache"],
        "A_v_cache": _A_in["v_cache"],
        "A_wq": _A_in["wq"],
        "A_wo": _A_in["wo"],
        "B_x": _B_in["x"],
        "B_w_gate": _B_in["w_gate"],
        "B_w_up": _B_in["w_up"],
        "B_w_down": _B_in["w_down"],
    }

def reference(A_x, A_k_cache, A_v_cache, A_wq, A_wo, B_x, B_w_gate, B_w_up, B_w_down):
    def _A_ref(x, k_cache, v_cache, wq, wo):
        B, Sq, D = x.shape
        H, Dh = 32, 128
        q = (lambda x, *d: x.permute(*d))((x @ wq).reshape(B, Sq, H, Dh), 0, 2, 1, 3)
        scores = torch.einsum("bhqd,bhkd->bhqk", q, k_cache) * (Dh ** -0.5)
        p = _softmax(scores, axis=-1)
        o = torch.einsum("bhqk,bhkd->bhqd", p, v_cache)
        return (lambda x, *d: x.permute(*d))(o, 0, 2, 1, 3).reshape(B, Sq, H * Dh) @ wo

    def _B_ref(x, w_gate, w_up, w_down):
        return (_gelu(x @ w_gate) * (x @ w_up)) @ w_down

    x = A_x
    h = x + _A_ref(_layernorm(x, axis=-1), A_k_cache, A_v_cache, A_wq, A_wo)
    h2 = h + _B_ref(_layernorm(h, axis=-1), B_w_gate, B_w_up, B_w_down)
    return h2
```

```cpp
#include <hip/hip_runtime.h>
#include <hip/hip_bf16.h>
#include <stdint.h>

typedef unsigned short u16;
typedef unsigned int u32;
using u32x4 = __attribute__((ext_vector_type(4))) unsigned;
using bf16x2 = __attribute__((ext_vector_type(2))) __bf16;

constexpr int DM = 512;
constexpr int NH = 32;
constexpr int SK = 16384;
constexpr int DH = 128;
constexpr int FF = 1792;
constexpr int NSPLIT = 8;
constexpr int NWG = NH * NSPLIT;
constexpr int KPS = SK / NSPLIT;

__device__ __forceinline__ float bf2f(u32 b16) { return __builtin_bit_cast(float, b16 << 16); }
__device__ __forceinline__ float rbf(float x) {
    __bf16 b = (__bf16)x;
    return (float)b;
}
__device__ __forceinline__ u32 f2bf_bits(float x) {
    __bf16 b = (__bf16)x;
    return (u32)__builtin_bit_cast(unsigned short, b);
}
__device__ __forceinline__ float dot2(u32 a, u32 b, float acc) {
    return __builtin_amdgcn_fdot2_f32_bf16(__builtin_bit_cast(bf16x2, a), __builtin_bit_cast(bf16x2, b), acc, false);
}
template <int CTRL> __device__ __forceinline__ float dpp(float x) {
    return __builtin_bit_cast(float, __builtin_amdgcn_update_dpp(0, __builtin_bit_cast(int, x), CTRL, 0xf, 0xf, true));
}
constexpr int ROR8 = 0x128, ROR4 = 0x124, ROR2 = 0x122, ROR1 = 0x121;
__device__ __forceinline__ float row16_sum(float x) {
    x += dpp<ROR8>(x); x += dpp<ROR4>(x); x += dpp<ROR2>(x); x += dpp<ROR1>(x);
    return x;
}

__device__ __forceinline__ float block_sum512(float v, float* red, int tid) {
    for (int o = 32; o >= 1; o >>= 1) v += __shfl_xor(v, o);
    __syncthreads();
    if ((tid & 63) == 0) red[tid >> 6] = v;
    __syncthreads();
    float s = 0.f;
#pragma unroll
    for (int i = 0; i < 8; ++i) s += red[i];
    return s;
}

__device__ __forceinline__ float layernorm512(float xv, float* red, int tid) {
    float s = block_sum512(xv, red, tid);
    float mean = s * (1.0f / 512.0f);
    float m = rbf(mean);
    float d = xv - mean;
    float ss = block_sum512(d * d, red, tid);
    float var = rbf(ss * (1.0f / 512.0f));
    float num = rbf(xv - m);
    float den = rbf(sqrtf(rbf(var + 1e-5f)));
    return rbf(num / den);
}

constexpr int K1_THREADS = 512;
constexpr int K1_WAVES = 8;
constexpr int NB = 8;
__global__ void __launch_bounds__(K1_THREADS, 2) attn_kernel(const u16* __restrict__ x, const u16* __restrict__ kc, const u16* __restrict__ vc,
                                                              const u16* __restrict__ wq, float* __restrict__ pm, float* __restrict__ pl, float* __restrict__ po) {
    __shared__ __attribute__((aligned(16))) float lds[32 * 132 + 64];
    const int tid = threadIdx.x, lane = tid & 63, wave = __builtin_amdgcn_readfirstlane(tid >> 6), r = lane >> 4, c = lane & 15;
    const int b = blockIdx.x;
    const int xcd = b & 7, j = b >> 3;
    const int h = xcd + 8 * (j >> 3), sp = j & 7;

    float* red = lds + 32 * 132;
    float xv = bf2f(x[tid]);
    float ln = layernorm512(xv, red, tid);
    __syncthreads();
    lds[tid] = ln;
    __syncthreads();
    float qa[8];
#pragma unroll
    for (int i = 0; i < 8; ++i) qa[i] = 0.f;
    {
        const u16* wp = wq + (size_t)(wave * 64 + r) * (NH * DH) + h * DH + 8 * c;
#pragma unroll
        for (int i = 0; i < 16; ++i) {
            u32x4 w = *reinterpret_cast<const u32x4*>(wp + (size_t)i * 4 * (NH * DH));
            float lv = lds[wave * 64 + 4 * i + r];
#pragma unroll
            for (int k = 0; k < 4; ++k) {
                qa[2 * k] = fmaf(lv, __builtin_bit_cast(float, w[k] << 16), qa[2 * k]);
                qa[2 * k + 1] = fmaf(lv, __builtin_bit_cast(float, w[k] & 0xffff0000u), qa[2 * k + 1]);
            }
        }
    }
    __syncthreads();
    {
        float* dst = lds + (wave * 4 + r) * 132 + 8 * c;
        *reinterpret_cast<float4*>(dst) = make_float4(qa[0], qa[1], qa[2], qa[3]);
        *reinterpret_cast<float4*>(dst + 4) = make_float4(qa[4], qa[5], qa[6], qa[7]);
    }
    __syncthreads();
    float qsum = 0.f;
    if (tid < 128) {
#pragma unroll
        for (int g = 0; g < 32; ++g) qsum += lds[g * 132 + tid];
    }
    __syncthreads();
    if (tid < 128) reinterpret_cast<u16*>(lds)[tid] = (u16)f2bf_bits(qsum);
    __syncthreads();
    const u32x4 qpk = *reinterpret_cast<const u32x4*>(reinterpret_cast<const u16*>(lds) + 8 * c);
    __syncthreads();

    const u16* kbase = kc + ((size_t)h * SK + (size_t)sp * KPS) * DH + (size_t)r * DH + 8 * c;
    const u16* vbase = vc + ((size_t)h * SK + (size_t)sp * KPS) * DH + (size_t)r * DH + 8 * c;
    constexpr int NIT = KPS / (4 * NB * K1_WAVES);
    float m = -INFINITY, l = 0.f, acc[8];
#pragma unroll
    for (int i = 0; i < 8; ++i) acc[i] = 0.f;
    constexpr float LOG2E = 1.4426950408889634f;
    constexpr float SCALE = 0.08838834764831845f;
    for (int it = 0; it < NIT; ++it) {
        const size_t off = (size_t)((it * K1_WAVES + wave) * (4 * NB)) * DH;
        u32x4 K[NB], V[NB];
#pragma unroll
        for (int t = 0; t < NB; ++t) K[t] = __builtin_nontemporal_load(reinterpret_cast<const u32x4*>(kbase + off + (size_t)t * 4 * DH));
#pragma unroll
        for (int t = 0; t < NB; ++t) V[t] = __builtin_nontemporal_load(reinterpret_cast<const u32x4*>(vbase + off + (size_t)t * 4 * DH));
        float s[NB];
        float mb = -INFINITY;
#pragma unroll
        for (int t = 0; t < NB; ++t) {
            float d = 0.f;
#pragma unroll
            for (int k = 0; k < 4; ++k) d = dot2(K[t][k], qpk[k], d);
            d = row16_sum(d);
            d = rbf(rbf(d) * SCALE);
            s[t] = d;
            mb = fmaxf(mb, d);
        }
        const float mn = fmaxf(m, mb);
        const float alpha = __builtin_amdgcn_exp2f((m - mn) * LOG2E);
        m = mn;
        l *= alpha;
#pragma unroll
        for (int i = 0; i < 8; ++i) acc[i] *= alpha;
#pragma unroll
        for (int t = 0; t < NB; ++t) {
            const float p = __builtin_amdgcn_exp2f((s[t] - mn) * LOG2E);
            l += p;
#pragma unroll
            for (int k = 0; k < 4; ++k) {
                acc[2 * k] = fmaf(p, __builtin_bit_cast(float, V[t][k] << 16), acc[2 * k]);
                acc[2 * k + 1] = fmaf(p, __builtin_bit_cast(float, V[t][k] & 0xffff0000u), acc[2 * k + 1]);
            }
        }
    }
    {
        float* dst = lds + (wave * 4 + r) * 132;
        *reinterpret_cast<float4*>(dst + 8 * c) = make_float4(acc[0], acc[1], acc[2], acc[3]);
        *reinterpret_cast<float4*>(dst + 8 * c + 4) = make_float4(acc[4], acc[5], acc[6], acc[7]);
        if (c == 0) { dst[128] = m; dst[129] = l; }
    }
    __syncthreads();
    if (tid < 128) {
        float M = -INFINITY;
#pragma unroll
        for (int g = 0; g < 32; ++g) M = fmaxf(M, lds[g * 132 + 128]);
        float L = 0.f, o = 0.f;
#pragma unroll
        for (int g = 0; g < 32; ++g) {
            const float w = __builtin_amdgcn_exp2f((lds[g * 132 + 128] - M) * LOG2E);
            L = fmaf(w, lds[g * 132 + 129], L);
            o = fmaf(w, lds[g * 132 + tid], o);
        }
        const int slot = h * NSPLIT + sp;
        po[(size_t)slot * DH + tid] = o;
        if (tid == 0) { pm[slot] = M; pl[slot] = L; }
    }
}

__global__ void __launch_bounds__(256) wo_kernel(const u16* __restrict__ x, const u16* __restrict__ wo, const float* __restrict__ pm, const float* __restrict__ pl,
                                                  const float* __restrict__ po, float* __restrict__ hout) {
    __shared__ __attribute__((aligned(16))) float o[NH * DH];
    __shared__ float red[4][8];
    const int tid = threadIdx.x, lane = tid & 63, wave = tid >> 6;
    constexpr float LOG2E = 1.4426950408889634f;
    for (int idx = tid; idx < NH * DH; idx += 256) {
        const int h = idx >> 7, d = idx & 127;
        float M = -INFINITY;
#pragma unroll
        for (int s = 0; s < NSPLIT; ++s) M = fmaxf(M, pm[h * NSPLIT + s]);
        float L = 0.f, a = 0.f;
#pragma unroll
        for (int s = 0; s < NSPLIT; ++s) {
            const float w = __builtin_amdgcn_exp2f((pm[h * NSPLIT + s] - M) * LOG2E);
            L = fmaf(w, pl[h * NSPLIT + s], L);
            a = fmaf(w, po[(size_t)(h * NSPLIT + s) * DH + d], a);
        }
        o[idx] = rbf(a / L);
    }
    __syncthreads();
    const int col0 = blockIdx.x * 8;
    float acc[8];
#pragma unroll
    for (int i = 0; i < 8; ++i) acc[i] = 0.f;
#pragma unroll
    for (int i = 0; i < 16; ++i) {
        const int row = tid + 256 * i;
        u32x4 w = *reinterpret_cast<const u32x4*>(wo + (size_t)row * DM + col0);
        const float ov = o[row];
#pragma unroll
        for (int k = 0; k < 4; ++k) {
            acc[2 * k] = fmaf(ov, __builtin_bit_cast(float, w[k] << 16), acc[2 * k]);
            acc[2 * k + 1] = fmaf(ov, __builtin_bit_cast(float, w[k] & 0xffff0000u), acc[2 * k + 1]);
        }
    }
#pragma unroll
    for (int i = 0; i < 8; ++i) {
        float v = acc[i];
        for (int off = 32; off >= 1; off >>= 1) v += __shfl_xor(v, off);
        if (lane == 0) red[wave][i] = v;
    }
    __syncthreads();
    if (tid < 8) {
        float v = red[0][tid] + red[1][tid] + red[2][tid] + red[3][tid];
        hout[col0 + tid] = rbf(bf2f(x[col0 + tid]) + rbf(v));
    }
}

__global__ void __launch_bounds__(512) ffn1_kernel(const float* __restrict__ hin, const u16* __restrict__ wg, const u16* __restrict__ wu, float* __restrict__ act) {
    __shared__ __attribute__((aligned(16))) float ln[DM];
    __shared__ float red[16];
    __shared__ float red2[8][16];
    const int tid = threadIdx.x, lane = tid & 63, wave = tid >> 6;
    float hv = hin[tid];
    float lnv = layernorm512(hv, red, tid);
    ln[tid] = lnv;
    __syncthreads();
    const int col0 = blockIdx.x * 8;
    float ag[8], au[8];
#pragma unroll
    for (int i = 0; i < 8; ++i) { ag[i] = 0.f; au[i] = 0.f; }
    {
        const int row = tid;
        u32x4 w1 = *reinterpret_cast<const u32x4*>(wg + (size_t)row * FF + col0);
        u32x4 w2 = *reinterpret_cast<const u32x4*>(wu + (size_t)row * FF + col0);
        const float lv = ln[row];
#pragma unroll
        for (int k = 0; k < 4; ++k) {
            ag[2 * k] = fmaf(lv, __builtin_bit_cast(float, w1[k] << 16), ag[2 * k]);
            ag[2 * k + 1] = fmaf(lv, __builtin_bit_cast(float, w1[k] & 0xffff0000u), ag[2 * k + 1]);
            au[2 * k] = fmaf(lv, __builtin_bit_cast(float, w2[k] << 16), au[2 * k]);
            au[2 * k + 1] = fmaf(lv, __builtin_bit_cast(float, w2[k] & 0xffff0000u), au[2 * k + 1]);
        }
    }
#pragma unroll
    for (int i = 0; i < 8; ++i) {
        float v = ag[i], u = au[i];
        for (int off = 32; off >= 1; off >>= 1) { v += __shfl_xor(v, off); u += __shfl_xor(u, off); }
        if (lane == 0) { red2[wave][i] = v; red2[wave][8 + i] = u; }
    }
    __syncthreads();
    if (tid < 8) {
        float g = 0.f, u = 0.f;
#pragma unroll
        for (int w = 0; w < 8; ++w) { g += red2[w][tid]; u += red2[w][8 + tid]; }
        g = rbf(g); u = rbf(u);
        const float inner = 0.7978845608028654f * (g + 0.044715f * g * g * g);
        const float ge = rbf(0.5f * g * (1.0f + tanhf(inner)));
        act[col0 + tid] = rbf(ge * u);
    }
}

__global__ void __launch_bounds__(256) ffn2_kernel(const float* __restrict__ hin, const float* __restrict__ act, const u16* __restrict__ wd, u16* __restrict__ out) {
    __shared__ float red[4][8];
    const int tid = threadIdx.x, lane = tid & 63, wave = tid >> 6;
    const int col0 = blockIdx.x * 8;
    float acc[8];
#pragma unroll
    for (int i = 0; i < 8; ++i) acc[i] = 0.f;
#pragma unroll
    for (int i = 0; i < 7; ++i) {
        const int row = tid + 256 * i;
        u32x4 w = *reinterpret_cast<const u32x4*>(wd + (size_t)row * DM + col0);
        const float av = act[row];
#pragma unroll
        for (int k = 0; k < 4; ++k) {
            acc[2 * k] = fmaf(av, __builtin_bit_cast(float, w[k] << 16), acc[2 * k]);
            acc[2 * k + 1] = fmaf(av, __builtin_bit_cast(float, w[k] & 0xffff0000u), acc[2 * k + 1]);
        }
    }
#pragma unroll
    for (int i = 0; i < 8; ++i) {
        float v = acc[i];
        for (int off = 32; off >= 1; off >>= 1) v += __shfl_xor(v, off);
        if (lane == 0) red[wave][i] = v;
    }
    __syncthreads();
    if (tid < 8) {
        float v = red[0][tid] + red[1][tid] + red[2][tid] + red[3][tid];
        out[col0 + tid] = (u16)f2bf_bits(hin[col0 + tid] + rbf(v));
    }
}

extern "C" void kernel_launch(void* const* d_in, const int* in_sizes, int n_in,
                              void* d_out, int out_size, void* d_ws, size_t ws_size,
                              hipStream_t stream) {
    const u16* x = (const u16*)d_in[0];
    const u16* kc = (const u16*)d_in[1];
    const u16* vc = (const u16*)d_in[2];
    const u16* wq = (const u16*)d_in[3];
    const u16* wo = (const u16*)d_in[4];
    const u16* wg = (const u16*)d_in[6];
    const u16* wu = (const u16*)d_in[7];
    const u16* wd = (const u16*)d_in[8];
    float* ws = (float*)d_ws;
    float* pm = ws;
    float* pl = ws + 256;
    float* po = ws + 512;
    float* hbuf = ws + 512 + 256 * 128;
    float* act = hbuf + 512;
    attn_kernel<<<NWG, K1_THREADS, 0, stream>>>(x, kc, vc, wq, pm, pl, po);
    wo_kernel<<<DM / 8, 256, 0, stream>>>(x, wo, pm, pl, po, hbuf);
    ffn1_kernel<<<FF / 8, 512, 0, stream>>>(hbuf, wg, wu, act);
    ffn2_kernel<<<DM / 8, 256, 0, stream>>>(hbuf, act, wd, (u16*)d_out);
}
```

```cpp
#include <hip/hip_runtime.h>
#include <hip/hip_bf16.h>
#include <stdint.h>

typedef unsigned short u16;
typedef unsigned int u32;
typedef unsigned long long u64;
using u32x4 = __attribute__((ext_vector_type(4))) unsigned;
using bf16x2 = __attribute__((ext_vector_type(2))) __bf16;

constexpr int DM = 512;
constexpr int NH = 32;
constexpr int SK = 16384;
constexpr int DH = 128;
constexpr int FF = 1792;
constexpr int NSPLIT = 8;
constexpr int NWG = NH * NSPLIT;
constexpr int KPS = SK / NSPLIT;
constexpr float LOG2E = 1.4426950408889634f;

__device__ __forceinline__ float bf2f(u32 b16) { return __builtin_bit_cast(float, b16 << 16); }
__device__ __forceinline__ float rbf(float x) { __bf16 b = (__bf16)x; return (float)b; }
__device__ __forceinline__ u32 f2bf_bits(float x) { __bf16 b = (__bf16)x; return (u32)__builtin_bit_cast(unsigned short, b); }
__device__ __forceinline__ float lo16(u32 w) { return __builtin_bit_cast(float, w << 16); }
__device__ __forceinline__ float hi16(u32 w) { return __builtin_bit_cast(float, w & 0xffff0000u); }
__device__ __forceinline__ float dot2(u32 a, u32 b, float acc) {
    return __builtin_amdgcn_fdot2_f32_bf16(__builtin_bit_cast(bf16x2, a), __builtin_bit_cast(bf16x2, b), acc, false);
}
template <int CTRL> __device__ __forceinline__ float dpp(float x) {
    return __builtin_bit_cast(float, __builtin_amdgcn_update_dpp(0, __builtin_bit_cast(int, x), CTRL, 0xf, 0xf, true));
}
__device__ __forceinline__ float row16_sum(float x) {
    x += dpp<0x128>(x); x += dpp<0x124>(x); x += dpp<0x122>(x); x += dpp<0x121>(x);
    return x;
}
template <int CTRL, int RM> __device__ __forceinline__ float dppm(float x) {
    return __builtin_bit_cast(float, __builtin_amdgcn_update_dpp(0, __builtin_bit_cast(int, x), CTRL, RM, 0xf, true));
}
__device__ __forceinline__ float wave_sum63(float x) {
    x += dppm<0x111, 0xf>(x); x += dppm<0x112, 0xf>(x); x += dppm<0x114, 0xf>(x); x += dppm<0x118, 0xf>(x);
    x += dppm<0x142, 0xa>(x); x += dppm<0x143, 0xc>(x);
    return x;
}
__device__ __forceinline__ void lds_barrier() { asm volatile("s_waitcnt lgkmcnt(0)\n\ts_barrier" ::: "memory"); }
__device__ __forceinline__ u32x4 ldnt(const u16* p) { return __builtin_nontemporal_load(reinterpret_cast<const u32x4*>(p)); }

template <int NWAVES>
__device__ __forceinline__ float block_sum(float v, float* red, int tid) {
    v = wave_sum63(v);
    lds_barrier();
    if ((tid & 63) == 63) red[tid >> 6] = v;
    lds_barrier();
    float s = 0.f;
#pragma unroll
    for (int i = 0; i < NWAVES; ++i) s += red[i];
    return s;
}

constexpr int K1_WAVES = 8;
constexpr int K1_THREADS = K1_WAVES * 64;
constexpr int NB = 4;
__global__ void __launch_bounds__(K1_THREADS, 2) attn_kernel(const u16* __restrict__ x, const u16* __restrict__ kc, const u16* __restrict__ vc,
                                                              const u16* __restrict__ wq, float* __restrict__ pm, float* __restrict__ pl, float* __restrict__ po,
                                                              u64* __restrict__ gran, int ngran) {
    __shared__ __attribute__((aligned(16))) float lds[32 * 132 + 64];
    const int tid = threadIdx.x, lane = tid & 63, wave = __builtin_amdgcn_readfirstlane(tid >> 6), r = lane >> 4, c = lane & 15;
    const int b = blockIdx.x;
    const int xcd = b & 7, j = b >> 3;
    const int h = xcd + 8 * (j >> 3), sp = j & 7;

    if (b < 4) { for (int i = b * K1_THREADS + tid; i < ngran; i += 4 * K1_THREADS) gran[i] = 0ull; }

    u32x4 w[16];
    {
        const u16* wp = wq + (size_t)(wave * 64 + r) * (NH * DH) + h * DH + 8 * c;
#pragma unroll
        for (int i = 0; i < 16; ++i) w[i] = *reinterpret_cast<const u32x4*>(wp + (size_t)i * 4 * (NH * DH));
    }
    const u16* kbase = kc + ((size_t)h * SK + (size_t)sp * KPS) * DH + (size_t)r * DH + 8 * c;
    const u16* vbase = vc + ((size_t)h * SK + (size_t)sp * KPS) * DH + (size_t)r * DH + 8 * c;
    constexpr int NIT = KPS / (4 * NB * K1_WAVES);
    u32x4 KA[NB], VA[NB], KB[NB], VB[NB], KC[NB], VC[NB], KD[NB], VD[NB];
#define K1_ISSUE(KX, VX, IT) do { const size_t off_ = (size_t)(((IT) * K1_WAVES + wave) * (4 * NB)) * DH; \
    _Pragma("unroll") for (int t = 0; t < NB; ++t) KX[t] = ldnt(kbase + off_ + (size_t)t * 4 * DH); \
    _Pragma("unroll") for (int t = 0; t < NB; ++t) VX[t] = ldnt(vbase + off_ + (size_t)t * 4 * DH); } while (0)
    K1_ISSUE(KA, VA, 0);
    K1_ISSUE(KB, VB, 1);
    K1_ISSUE(KC, VC, 2);
    K1_ISSUE(KD, VD, 3);

    float* red = lds + 32 * 132;
    float xv = (tid < DM) ? bf2f(x[tid]) : 0.f;
    {
        float s = block_sum<K1_WAVES>(xv, red, tid);
        float mean = s * (1.0f / 512.0f);
        float mbf = rbf(mean);
        float d = (tid < DM) ? (xv - mean) : 0.f;
        float ss = block_sum<K1_WAVES>(d * d, red, tid);
        float var = rbf(ss * (1.0f / 512.0f));
        float num = rbf(xv - mbf);
        float den = rbf(sqrtf(rbf(var + 1e-5f)));
        float ln = rbf(num / den);
        __syncthreads();
        if (tid < DM) lds[tid] = ln;
        __syncthreads();
    }
    float qa[8];
#pragma unroll
    for (int i = 0; i < 8; ++i) qa[i] = 0.f;
#pragma unroll
    for (int i = 0; i < 16; ++i) {
        const float lv = lds[wave * 64 + 4 * i + r];
#pragma unroll
        for (int k = 0; k < 4; ++k) {
            qa[2 * k] = fmaf(lv, lo16(w[i][k]), qa[2 * k]);
            qa[2 * k + 1] = fmaf(lv, hi16(w[i][k]), qa[2 * k + 1]);
        }
    }
    __syncthreads();
    {
        float* dst = lds + (wave * 4 + r) * 132 + 8 * c;
        *reinterpret_cast<float4*>(dst) = make_float4(qa[0], qa[1], qa[2], qa[3]);
        *reinterpret_cast<float4*>(dst + 4) = make_float4(qa[4], qa[5], qa[6], qa[7]);
    }
    __syncthreads();
    float qsum = 0.f;
    if (tid < 128) {
#pragma unroll
        for (int g = 0; g < 32; ++g) qsum += lds[g * 132 + tid];
    }
    __syncthreads();
    if (tid < 128) reinterpret_cast<u16*>(lds)[tid] = (u16)f2bf_bits(qsum);
    __syncthreads();
    const u32x4 qpk = *reinterpret_cast<const u32x4*>(reinterpret_cast<const u16*>(lds) + 8 * c);

    float m = -INFINITY, l = 0.f, acc[8];
#pragma unroll
    for (int i = 0; i < 8; ++i) acc[i] = 0.f;
    constexpr float SCALE = 0.08838834764831845f;
#define K1_COMPUTE(KX, VX) do { \
        float s_[NB]; float mb_ = -INFINITY; \
        _Pragma("unroll") for (int t = 0; t < NB; ++t) { float d_ = 0.f; \
            _Pragma("unroll") for (int k = 0; k < 4; ++k) d_ = dot2(KX[t][k], qpk[k], d_); \
            d_ = row16_sum(d_); d_ = rbf(rbf(d_) * SCALE); s_[t] = d_; mb_ = fmaxf(mb_, d_); } \
        const float mn_ = fmaxf(m, mb_); \
        const float alpha_ = __builtin_amdgcn_exp2f((m - mn_) * LOG2E); \
        m = mn_; l *= alpha_; \
        _Pragma("unroll") for (int i = 0; i < 8; ++i) acc[i] *= alpha_; \
        _Pragma("unroll") for (int t = 0; t < NB; ++t) { \
            const float p_ = __builtin_amdgcn_exp2f((s_[t] - mn_) * LOG2E); l += p_; \
            _Pragma("unroll") for (int k = 0; k < 4; ++k) { \
                acc[2 * k] = fmaf(p_, lo16(VX[t][k]), acc[2 * k]); \
                acc[2 * k + 1] = fmaf(p_, hi16(VX[t][k]), acc[2 * k + 1]); } } } while (0)
    K1_COMPUTE(KA, VA);
    K1_ISSUE(KA, VA, 4);
    K1_COMPUTE(KB, VB);
    K1_ISSUE(KB, VB, 5);
    K1_COMPUTE(KC, VC);
    K1_COMPUTE(KD, VD);
#pragma unroll 1
    for (int it = 4; it < NIT; it += 2) {
        K1_COMPUTE(KA, VA);
        if (it + 2 < NIT) K1_ISSUE(KA, VA, it + 2);
        K1_COMPUTE(KB, VB);
        if (it + 3 < NIT) K1_ISSUE(KB, VB, it + 3);
    }
    __syncthreads();
    {
        float* dst = lds + (wave * 4 + r) * 132;
        *reinterpret_cast<float4*>(dst + 8 * c) = make_float4(acc[0], acc[1], acc[2], acc[3]);
        *reinterpret_cast<float4*>(dst + 8 * c + 4) = make_float4(acc[4], acc[5], acc[6], acc[7]);
        if (c == 0) { dst[128] = m; dst[129] = l; }
    }
    __syncthreads();
    if (tid < 128) {
        float M = -INFINITY;
#pragma unroll
        for (int g = 0; g < 32; ++g) M = fmaxf(M, lds[g * 132 + 128]);
        float L = 0.f, o = 0.f;
#pragma unroll
        for (int g = 0; g < 32; ++g) {
            const float wgt = __builtin_amdgcn_exp2f((lds[g * 132 + 128] - M) * LOG2E);
            L = fmaf(wgt, lds[g * 132 + 129], L);
            o = fmaf(wgt, lds[g * 132 + tid], o);
        }
        const int slot = h * NSPLIT + sp;
        po[((size_t)h * DH + tid) * NSPLIT + sp] = o;
        if (tid == 0) { pm[slot] = M; pl[slot] = L; }
    }
}

#define RLX_AGENT __ATOMIC_RELAXED, __HIP_MEMORY_SCOPE_AGENT
constexpr u32 TAG = 1u;
constexpr int SPIN_CAP = 1 << 22;
__device__ __forceinline__ void publish(u64* g, float v) {
    __hip_atomic_store(g, ((u64)TAG << 32) | (u64)__builtin_bit_cast(u32, v), RLX_AGENT);
}
template <int N>
__device__ __forceinline__ void gather(const u64* g, float* dst, int lane) {
    for (int spin = 0; spin < SPIN_CAP; ++spin) {
        u64 v[N];
        bool ok = true;
#pragma unroll
        for (int k = 0; k < N; ++k) v[k] = __hip_atomic_load(const_cast<u64*>(g) + lane + 64 * k, RLX_AGENT);
#pragma unroll
        for (int k = 0; k < N; ++k) { ok &= ((u32)(v[k] >> 32) == TAG); dst[lane + 64 * k] = __builtin_bit_cast(float, (u32)v[k]); }
        if (__all(ok)) break;
    }
}

constexpr int NRB = 8;
constexpr int NCBA = 16;
constexpr int NWA = NRB * NCBA;
constexpr int NWB = FF / 32;
constexpr int NWC = DM / 8;
constexpr int K2_GRID = NWA + NWB + NWC;
__global__ void __launch_bounds__(256) tail_kernel(const u16* __restrict__ x, const u16* __restrict__ wo, const u16* __restrict__ wg, const u16* __restrict__ wu,
                                                    const u16* __restrict__ wd, const float* __restrict__ pm, const float* __restrict__ pl, const float* __restrict__ po,
                                                    u64* __restrict__ gh, u64* __restrict__ gact, u16* __restrict__ out) {
    __shared__ __attribute__((aligned(16))) float hp[NRB * DM];
    __shared__ __attribute__((aligned(16))) float as[FF];
    __shared__ __attribute__((aligned(16))) float ln[DM];
    __shared__ __attribute__((aligned(16))) float f[NH * NSPLIT];
    __shared__ float red[4][64];
    const int tid = threadIdx.x, lane = tid & 63, wave = tid >> 6;
    const int b = blockIdx.x;
    if (b < NWA) {
        const int rb = b & 7, cb = b >> 3;
        float4 pv[2][2];
#pragma unroll
        for (int i = 0; i < 2; ++i) {
            const int row = rb * 512 + tid + 256 * i;
            pv[i][0] = *reinterpret_cast<const float4*>(po + (size_t)row * NSPLIT);
            pv[i][1] = *reinterpret_cast<const float4*>(po + (size_t)row * NSPLIT + 4);
        }
        u32x4 wA[8];
#pragma unroll
        for (int i = 0; i < 8; ++i) wA[i] = *reinterpret_cast<const u32x4*>(wo + (size_t)(rb * 512 + (wave * 8 + i) * 16 + (lane >> 2)) * DM + cb * 32 + 8 * (lane & 3));
        const float pmv = pm[tid], plv = pl[tid];
        {
            float M = pmv;
            M = fmaxf(M, dpp<0xB1>(M)); M = fmaxf(M, dpp<0x4E>(M)); M = fmaxf(M, dpp<0x141>(M));
            const float wv = __builtin_amdgcn_exp2f((pmv - M) * LOG2E);
            float L = wv * plv;
            L += dpp<0xB1>(L); L += dpp<0x4E>(L); L += dpp<0x141>(L);
            f[tid] = wv / L;
        }
        lds_barrier();
#pragma unroll
        for (int i = 0; i < 2; ++i) {
            const int row = rb * 512 + tid + 256 * i;
            const int hh = row >> 7;
            const float4 f0 = *reinterpret_cast<const float4*>(f + hh * NSPLIT), f1 = *reinterpret_cast<const float4*>(f + hh * NSPLIT + 4);
            float o = 0.f;
            o = fmaf(f0.x, pv[i][0].x, o); o = fmaf(f0.y, pv[i][0].y, o); o = fmaf(f0.z, pv[i][0].z, o); o = fmaf(f0.w, pv[i][0].w, o);
            o = fmaf(f1.x, pv[i][1].x, o); o = fmaf(f1.y, pv[i][1].y, o); o = fmaf(f1.z, pv[i][1].z, o); o = fmaf(f1.w, pv[i][1].w, o);
            ln[tid + 256 * i] = rbf(o);
        }
        lds_barrier();
        float acc[8];
#pragma unroll
        for (int i = 0; i < 8; ++i) acc[i] = 0.f;
#pragma unroll
        for (int i = 0; i < 8; ++i) {
            const float o = ln[(wave * 8 + i) * 16 + (lane >> 2)];
#pragma unroll
            for (int k = 0; k < 4; ++k) {
                acc[2 * k] = fmaf(o, lo16(wA[i][k]), acc[2 * k]);
                acc[2 * k + 1] = fmaf(o, hi16(wA[i][k]), acc[2 * k + 1]);
            }
        }
#pragma unroll
        for (int i = 0; i < 8; ++i) {
            float v = acc[i];
            v += dpp<0x124>(v); v += dpp<0x128>(v);
            if ((lane & 15) < 4) hp[((wave * 4 + (lane >> 4)) * 4 + (lane & 3)) * 8 + i] = v;
        }
        lds_barrier();
        if (tid < 32) {
            float v = 0.f;
#pragma unroll
            for (int g = 0; g < 16; ++g) v += hp[g * 32 + tid];
            publish(gh + rb * DM + cb * 32 + tid, v);
        }
        return;
    }
    if (b < NWA + NWB) {
        const int col0 = (b - NWA) * 32;
        u32x4 wG[8], wU[8];
#pragma unroll
        for (int i = 0; i < 8; ++i) {
            const size_t off = (size_t)((wave * 8 + i) * 16 + (lane >> 2)) * FF + col0 + 8 * (lane & 3);
            wG[i] = *reinterpret_cast<const u32x4*>(wg + off);
            wU[i] = *reinterpret_cast<const u32x4*>(wu + off);
        }
        const float x0 = bf2f(x[tid]), x1 = bf2f(x[tid + 256]);
        gather<16>(gh + wave * 2 * DM, hp + wave * 2 * DM, lane);
        lds_barrier();
        float h0 = 0.f, h1 = 0.f;
#pragma unroll
        for (int q = 0; q < NRB; ++q) { h0 += hp[q * DM + tid]; h1 += hp[q * DM + tid + 256]; }
        h0 = rbf(x0 + rbf(h0)); h1 = rbf(x1 + rbf(h1));
        {
            float s = block_sum<4>(h0 + h1, red[0], tid);
            const float mean = s * (1.0f / 512.0f);
            const float mbf = rbf(mean);
            const float d0 = h0 - mean, d1 = h1 - mean;
            float ss = block_sum<4>(d0 * d0 + d1 * d1, red[0], tid);
            const float var = rbf(ss * (1.0f / 512.0f));
            const float den = rbf(sqrtf(rbf(var + 1e-5f)));
            ln[tid] = rbf(rbf(h0 - mbf) / den);
            ln[tid + 256] = rbf(rbf(h1 - mbf) / den);
        }
        lds_barrier();
        float acc[16];
#pragma unroll
        for (int i = 0; i < 16; ++i) acc[i] = 0.f;
#pragma unroll
        for (int i = 0; i < 8; ++i) {
            const float lv = ln[(wave * 8 + i) * 16 + (lane >> 2)];
#pragma unroll
            for (int k = 0; k < 4; ++k) {
                acc[2 * k] = fmaf(lv, lo16(wG[i][k]), acc[2 * k]);
                acc[2 * k + 1] = fmaf(lv, hi16(wG[i][k]), acc[2 * k + 1]);
                acc[8 + 2 * k] = fmaf(lv, lo16(wU[i][k]), acc[8 + 2 * k]);
                acc[8 + 2 * k + 1] = fmaf(lv, hi16(wU[i][k]), acc[8 + 2 * k + 1]);
            }
        }
#pragma unroll
        for (int i = 0; i < 16; ++i) {
            float v = acc[i];
            v += dpp<0x124>(v); v += dpp<0x128>(v);
            if ((lane & 15) < 4) hp[((wave * 4 + (lane >> 4)) * 4 + (lane & 3)) * 16 + i] = v;
        }
        lds_barrier();
        if (tid < 32) {
            float g = 0.f, u = 0.f;
#pragma unroll
            for (int q = 0; q < 16; ++q) { g += hp[(q * 4 + (tid >> 3)) * 16 + (tid & 7)]; u += hp[(q * 4 + (tid >> 3)) * 16 + 8 + (tid & 7)]; }
            g = rbf(g); u = rbf(u);
            const float inner = 0.7978845608028654f * (g + 0.044715f * g * g * g);
            const float ge = rbf(0.5f * g * (1.0f + tanhf(inner)));
            publish(gact + col0 + tid, rbf(ge * u));
        }
        return;
    }
    if (b < K2_GRID) {
        const int col0 = (b - NWA - NWB) * 8;
        u32x4 wD[7];
#pragma unroll
        for (int i = 0; i < 7; ++i) wD[i] = *reinterpret_cast<const u32x4*>(wd + (size_t)(tid + 256 * i) * DM + col0);
        const float xv = bf2f(x[col0 + (tid & 7)]);
        if (wave == 0) {
            const u64* g = gh + (lane >> 3) * DM + col0 + (lane & 7);
            u64 v;
            for (int spin = 0; spin < SPIN_CAP; ++spin) {
                v = __hip_atomic_load(const_cast<u64*>(g), RLX_AGENT);
                if (__all((u32)(v >> 32) == TAG)) break;
            }
            float hpart = __builtin_bit_cast(float, (u32)v);
            hpart += dpp<0x128>(hpart);
            hp[lane] = hpart;
        }
        gather<7>(gact + wave * 448, as + wave * 448, lane);
        lds_barrier();
        float acc[8];
#pragma unroll
        for (int i = 0; i < 8; ++i) acc[i] = 0.f;
#pragma unroll
        for (int i = 0; i < 7; ++i) {
            const float av = as[tid + 256 * i];
#pragma unroll
            for (int k = 0; k < 4; ++k) {
                acc[2 * k] = fmaf(av, lo16(wD[i][k]), acc[2 * k]);
                acc[2 * k + 1] = fmaf(av, hi16(wD[i][k]), acc[2 * k + 1]);
            }
        }
#pragma unroll
        for (int i = 0; i < 8; ++i) {
            const float v = wave_sum63(acc[i]);
            if (lane == 63) red[wave][i] = v;
        }
        lds_barrier();
        if (tid < 8) {
            const float v = red[0][tid] + red[1][tid] + red[2][tid] + red[3][tid];
            const float hsum = (hp[tid] + hp[16 + tid]) + (hp[32 + tid] + hp[48 + tid]);
            const float hv = rbf(xv + rbf(hsum));
            out[col0 + tid] = (u16)f2bf_bits(hv + rbf(v));
        }
    }
}

extern "C" void kernel_launch(void* const* d_in, const int* in_sizes, int n_in,
                              void* d_out, int out_size, void* d_ws, size_t ws_size,
                              hipStream_t stream) {
    const u16* x = (const u16*)d_in[0];
    const u16* kc = (const u16*)d_in[1];
    const u16* vc = (const u16*)d_in[2];
    const u16* wq = (const u16*)d_in[3];
    const u16* wo = (const u16*)d_in[4];
    const u16* wg = (const u16*)d_in[6];
    const u16* wu = (const u16*)d_in[7];
    const u16* wd = (const u16*)d_in[8];
    float* ws = (float*)d_ws;
    float* pm = ws;
    float* pl = ws + 256;
    float* po = ws + 512;
    u64* gh = (u64*)(ws + 512 + 256 * 128);
    u64* gact = gh + NRB * DM;
    attn_kernel<<<NWG, K1_THREADS, 0, stream>>>(x, kc, vc, wq, pm, pl, po, gh, NRB * DM + FF);
    tail_kernel<<<K2_GRID, 256, 0, stream>>>(x, wo, wg, wu, wd, pm, pl, po, gh, gact, (u16*)d_out);
}
```

```cpp
#include <hip/hip_runtime.h>
#include <hip/hip_bf16.h>
#include <stdint.h>

typedef unsigned short u16;
typedef unsigned int u32;
typedef unsigned long long u64;
using u32x4 = __attribute__((ext_vector_type(4))) unsigned;
using bf16x2 = __attribute__((ext_vector_type(2))) __bf16;

constexpr int DM = 512;
constexpr int NH = 32;
constexpr int SK = 16384;
constexpr int DH = 128;
constexpr int FF = 1792;
constexpr int NSPLIT = 8;
constexpr int NWG = NH * NSPLIT;
constexpr int KPS = SK / NSPLIT;
constexpr float LOG2E = 1.4426950408889634f;

__device__ __forceinline__ float bf2f(u32 b16) { return __builtin_bit_cast(float, b16 << 16); }
__device__ __forceinline__ float rbf(float x) { __bf16 b = (__bf16)x; return (float)b; }
__device__ __forceinline__ u32 f2bf_bits(float x) { __bf16 b = (__bf16)x; return (u32)__builtin_bit_cast(unsigned short, b); }
__device__ __forceinline__ float lo16(u32 w) { return __builtin_bit_cast(float, w << 16); }
__device__ __forceinline__ float hi16(u32 w) { return __builtin_bit_cast(float, w & 0xffff0000u); }
__device__ __forceinline__ float dot2(u32 a, u32 b, float acc) {
    return __builtin_amdgcn_fdot2_f32_bf16(__builtin_bit_cast(bf16x2, a), __builtin_bit_cast(bf16x2, b), acc, false);
}
template <int CTRL> __device__ __forceinline__ float dpp(float x) {
    return __builtin_bit_cast(float, __builtin_amdgcn_update_dpp(0, __builtin_bit_cast(int, x), CTRL, 0xf, 0xf, true));
}
__device__ __forceinline__ float row16_sum(float x) {
    x += dpp<0x128>(x); x += dpp<0x124>(x); x += dpp<0x122>(x); x += dpp<0x121>(x);
    return x;
}
template <int CTRL, int RM> __device__ __forceinline__ float dppm(float x) {
    return __builtin_bit_cast(float, __builtin_amdgcn_update_dpp(0, __builtin_bit_cast(int, x), CTRL, RM, 0xf, true));
}
__device__ __forceinline__ float wave_sum63(float x) {
    x += dppm<0x111, 0xf>(x); x += dppm<0x112, 0xf>(x); x += dppm<0x114, 0xf>(x); x += dppm<0x118, 0xf>(x);
    x += dppm<0x142, 0xa>(x); x += dppm<0x143, 0xc>(x);
    return x;
}
__device__ __forceinline__ void lds_barrier() { asm volatile("s_waitcnt lgkmcnt(0)\n\ts_barrier" ::: "memory"); }
__device__ __forceinline__ u32x4 ldnt(const u16* p) { return __builtin_nontemporal_load(reinterpret_cast<const u32x4*>(p)); }

template <int NWAVES>
__device__ __forceinline__ float block_sum(float v, float* red, int tid) {
    v = wave_sum63(v);
    lds_barrier();
    if ((tid & 63) == 63) red[tid >> 6] = v;
    lds_barrier();
    float s = 0.f;
#pragma unroll
    for (int i = 0; i < NWAVES; ++i) s += red[i];
    return s;
}

constexpr int K1_WAVES = 8;
constexpr int K1_THREADS = K1_WAVES * 64;
constexpr int NB = 4;
__global__ void __launch_bounds__(K1_THREADS, 2) attn_kernel(const u16* __restrict__ x, const u16* __restrict__ kc, const u16* __restrict__ vc,
                                                              const u16* __restrict__ wq, float* __restrict__ pm, float* __restrict__ pl, float* __restrict__ po,
                                                              u64* __restrict__ gran, int ngran) {
    __shared__ __attribute__((aligned(16))) float lds[32 * 132 + 64];
    const int tid = threadIdx.x, lane = tid & 63, wave = __builtin_amdgcn_readfirstlane(tid >> 6), r = lane >> 4, c = lane & 15;
    const int b = blockIdx.x;
    const int xcd = b & 7, j = b >> 3;
    const int h = xcd + 8 * (j >> 3), sp = j & 7;

    if (b < 4) { for (int i = b * K1_THREADS + tid; i < ngran; i += 4 * K1_THREADS) gran[i] = 0ull; }

    const float xv = bf2f(x[tid]);
    u32x4 w[16];
    {
        const u16* wp = wq + (size_t)(wave * 64 + r) * (NH * DH) + h * DH + 8 * c;
#pragma unroll
        for (int i = 0; i < 16; ++i) w[i] = *reinterpret_cast<const u32x4*>(wp + (size_t)i * 4 * (NH * DH));
    }
    const u16* kbase = kc + ((size_t)h * SK + (size_t)sp * KPS) * DH + (size_t)r * DH + 8 * c;
    const u16* vbase = vc + ((size_t)h * SK + (size_t)sp * KPS) * DH + (size_t)r * DH + 8 * c;
    constexpr int NIT = KPS / (4 * NB * K1_WAVES);
    u32x4 KA[NB], VA[NB], KB[NB], VB[NB], KC[NB], VC[NB], KD[NB], VD[NB];
#define K1_ISSUE(KX, VX, IT) do { const size_t off_ = (size_t)(((IT) * K1_WAVES + wave) * (4 * NB)) * DH; \
    _Pragma("unroll") for (int t = 0; t < NB; ++t) KX[t] = ldnt(kbase + off_ + (size_t)t * 4 * DH); \
    _Pragma("unroll") for (int t = 0; t < NB; ++t) VX[t] = ldnt(vbase + off_ + (size_t)t * 4 * DH); } while (0)
    K1_ISSUE(KA, VA, 0);
    K1_ISSUE(KB, VB, 1);
    K1_ISSUE(KC, VC, 2);
    K1_ISSUE(KD, VD, 3);

    float* red = lds + 32 * 132;
    {
        float s = block_sum<K1_WAVES>(xv, red, tid);
        float mean = s * (1.0f / 512.0f);
        float mbf = rbf(mean);
        float d = xv - mean;
        float ss = block_sum<K1_WAVES>(d * d, red, tid);
        float var = rbf(ss * (1.0f / 512.0f));
        float num = rbf(xv - mbf);
        float den = rbf(sqrtf(rbf(var + 1e-5f)));
        float ln = rbf(num / den);
        lds_barrier();
        lds[tid] = ln;
        lds_barrier();
    }
    float qa[8];
#pragma unroll
    for (int i = 0; i < 8; ++i) qa[i] = 0.f;
#pragma unroll
    for (int i = 0; i < 16; ++i) {
        const float lv = lds[wave * 64 + 4 * i + r];
#pragma unroll
        for (int k = 0; k < 4; ++k) {
            qa[2 * k] = fmaf(lv, lo16(w[i][k]), qa[2 * k]);
            qa[2 * k + 1] = fmaf(lv, hi16(w[i][k]), qa[2 * k + 1]);
        }
    }
    lds_barrier();
    {
        float* dst = lds + (wave * 4 + r) * 132 + 8 * c;
        *reinterpret_cast<float4*>(dst) = make_float4(qa[0], qa[1], qa[2], qa[3]);
        *reinterpret_cast<float4*>(dst + 4) = make_float4(qa[4], qa[5], qa[6], qa[7]);
    }
    lds_barrier();
    float qsum = 0.f;
    if (tid < 128) {
#pragma unroll
        for (int g = 0; g < 32; ++g) qsum += lds[g * 132 + tid];
    }
    lds_barrier();
    if (tid < 128) reinterpret_cast<u16*>(lds)[tid] = (u16)f2bf_bits(qsum);
    lds_barrier();
    const u32x4 qpk = *reinterpret_cast<const u32x4*>(reinterpret_cast<const u16*>(lds) + 8 * c);

    float m = -INFINITY, l = 0.f, acc[8];
#pragma unroll
    for (int i = 0; i < 8; ++i) acc[i] = 0.f;
    constexpr float SCALE = 0.08838834764831845f;
#define K1_COMPUTE(KX, VX) do { \
        float s_[NB]; float mb_ = -INFINITY; \
        _Pragma("unroll") for (int t = 0; t < NB; ++t) { float d_ = 0.f; \
            _Pragma("unroll") for (int k = 0; k < 4; ++k) d_ = dot2(KX[t][k], qpk[k], d_); \
            d_ = row16_sum(d_); d_ = rbf(rbf(d_) * SCALE); s_[t] = d_; mb_ = fmaxf(mb_, d_); } \
        const float mn_ = fmaxf(m, mb_); \
        const float alpha_ = __builtin_amdgcn_exp2f((m - mn_) * LOG2E); \
        m = mn_; l *= alpha_; \
        _Pragma("unroll") for (int i = 0; i < 8; ++i) acc[i] *= alpha_; \
        _Pragma("unroll") for (int t = 0; t < NB; ++t) { \
            const float p_ = __builtin_amdgcn_exp2f((s_[t] - mn_) * LOG2E); l += p_; \
            _Pragma("unroll") for (int k = 0; k < 4; ++k) { \
                acc[2 * k] = fmaf(p_, lo16(VX[t][k]), acc[2 * k]); \
                acc[2 * k + 1] = fmaf(p_, hi16(VX[t][k]), acc[2 * k + 1]); } } } while (0)
    K1_COMPUTE(KA, VA);
    K1_ISSUE(KA, VA, 4);
    K1_COMPUTE(KB, VB);
    K1_ISSUE(KB, VB, 5);
    K1_COMPUTE(KC, VC);
    K1_COMPUTE(KD, VD);
#pragma unroll 1
    for (int it = 4; it < NIT; it += 2) {
        K1_COMPUTE(KA, VA);
        if (it + 2 < NIT) K1_ISSUE(KA, VA, it + 2);
        K1_COMPUTE(KB, VB);
        if (it + 3 < NIT) K1_ISSUE(KB, VB, it + 3);
    }
    lds_barrier();
    {
        float* dst = lds + (wave * 4 + r) * 132;
        *reinterpret_cast<float4*>(dst + 8 * c) = make_float4(acc[0], acc[1], acc[2], acc[3]);
        *reinterpret_cast<float4*>(dst + 8 * c + 4) = make_float4(acc[4], acc[5], acc[6], acc[7]);
        if (c == 0) { dst[128] = m; dst[129] = l; }
    }
    lds_barrier();
    if (tid < 128) {
        float M = -INFINITY;
#pragma unroll
        for (int g = 0; g < 32; ++g) M = fmaxf(M, lds[g * 132 + 128]);
        float L = 0.f, o = 0.f;
#pragma unroll
        for (int g = 0; g < 32; ++g) {
            const float wgt = __builtin_amdgcn_exp2f((lds[g * 132 + 128] - M) * LOG2E);
            L = fmaf(wgt, lds[g * 132 + 129], L);
            o = fmaf(wgt, lds[g * 132 + tid], o);
        }
        const int slot = h * NSPLIT + sp;
        po[((size_t)h * DH + tid) * NSPLIT + sp] = o;
        if (tid == 0) { pm[slot] = M; pl[slot] = L; }
    }
}

#define RLX_AGENT __ATOMIC_RELAXED, __HIP_MEMORY_SCOPE_AGENT
constexpr u32 TAG = 1u;
constexpr int SPIN_CAP = 1 << 22;
__device__ __forceinline__ void publish(u64* g, float v) {
    __hip_atomic_store(g, ((u64)TAG << 32) | (u64)__builtin_bit_cast(u32, v), RLX_AGENT);
}
template <int N>
__device__ __forceinline__ void gather(const u64* g, float* dst, int lane) {
    for (int spin = 0; spin < SPIN_CAP; ++spin) {
        u64 v[N];
        bool ok = true;
#pragma unroll
        for (int k = 0; k < N; ++k) v[k] = __hip_atomic_load(const_cast<u64*>(g) + lane + 64 * k, RLX_AGENT);
#pragma unroll
        for (int k = 0; k < N; ++k) { ok &= ((u32)(v[k] >> 32) == TAG); dst[lane + 64 * k] = __builtin_bit_cast(float, (u32)v[k]); }
        if (__all(ok)) break;
    }
}

constexpr int NRB = 8;
constexpr int NCBA = 16;
constexpr int NWA = NRB * NCBA;
constexpr int NWB = FF / 32;
constexpr int NWC = DM / 8;
constexpr int K2_GRID = NWA + NWB + NWC;
__global__ void __launch_bounds__(256) tail_kernel(const u16* __restrict__ x, const u16* __restrict__ wo, const u16* __restrict__ wg, const u16* __restrict__ wu,
                                                    const u16* __restrict__ wd, const float* __restrict__ pm, const float* __restrict__ pl, const float* __restrict__ po,
                                                    u64* __restrict__ gh, u64* __restrict__ gact, u16* __restrict__ out) {
    __shared__ __attribute__((aligned(16))) float hp[NRB * DM];
    __shared__ __attribute__((aligned(16))) float as[FF];
    __shared__ __attribute__((aligned(16))) float ln[DM];
    __shared__ __attribute__((aligned(16))) float f[NH * NSPLIT];
    __shared__ float red[4][64];
    const int tid = threadIdx.x, lane = tid & 63, wave = tid >> 6;
    const int b = blockIdx.x;
    if (b < NWA) {
        const int rb = b & 7, cb = b >> 3;
        float4 pv[2][2];
#pragma unroll
        for (int i = 0; i < 2; ++i) {
            const int row = rb * 512 + tid + 256 * i;
            pv[i][0] = *reinterpret_cast<const float4*>(po + (size_t)row * NSPLIT);
            pv[i][1] = *reinterpret_cast<const float4*>(po + (size_t)row * NSPLIT + 4);
        }
        u32x4 wA[8];
#pragma unroll
        for (int i = 0; i < 8; ++i) wA[i] = *reinterpret_cast<const u32x4*>(wo + (size_t)(rb * 512 + (wave * 8 + i) * 16 + (lane >> 2)) * DM + cb * 32 + 8 * (lane & 3));
        const float pmv = pm[tid], plv = pl[tid];
        {
            float M = pmv;
            M = fmaxf(M, dpp<0xB1>(M)); M = fmaxf(M, dpp<0x4E>(M)); M = fmaxf(M, dpp<0x141>(M));
            const float wv = __builtin_amdgcn_exp2f((pmv - M) * LOG2E);
            float L = wv * plv;
            L += dpp<0xB1>(L); L += dpp<0x4E>(L); L += dpp<0x141>(L);
            f[tid] = wv / L;
        }
        lds_barrier();
#pragma unroll
        for (int i = 0; i < 2; ++i) {
            const int row = rb * 512 + tid + 256 * i;
            const int hh = row >> 7;
            const float4 f0 = *reinterpret_cast<const float4*>(f + hh * NSPLIT), f1 = *reinterpret_cast<const float4*>(f + hh * NSPLIT + 4);
            float o = 0.f;
            o = fmaf(f0.x, pv[i][0].x, o); o = fmaf(f0.y, pv[i][0].y, o); o = fmaf(f0.z, pv[i][0].z, o); o = fmaf(f0.w, pv[i][0].w, o);
            o = fmaf(f1.x, pv[i][1].x, o); o = fmaf(f1.y, pv[i][1].y, o); o = fmaf(f1.z, pv[i][1].z, o); o = fmaf(f1.w, pv[i][1].w, o);
            ln[tid + 256 * i] = rbf(o);
        }
        lds_barrier();
        float acc[8];
#pragma unroll
        for (int i = 0; i < 8; ++i) acc[i] = 0.f;
#pragma unroll
        for (int i = 0; i < 8; ++i) {
            const float o = ln[(wave * 8 + i) * 16 + (lane >> 2)];
#pragma unroll
            for (int k = 0; k < 4; ++k) {
                acc[2 * k] = fmaf(o, lo16(wA[i][k]), acc[2 * k]);
                acc[2 * k + 1] = fmaf(o, hi16(wA[i][k]), acc[2 * k + 1]);
            }
        }
#pragma unroll
        for (int i = 0; i < 8; ++i) {
            float v = acc[i];
            v += dpp<0x124>(v); v += dpp<0x128>(v);
            if ((lane & 15) < 4) hp[((wave * 4 + (lane >> 4)) * 4 + (lane & 3)) * 8 + i] = v;
        }
        lds_barrier();
        if (tid < 32) {
            float v = 0.f;
#pragma unroll
            for (int g = 0; g < 16; ++g) v += hp[g * 32 + tid];
            publish(gh + rb * DM + cb * 32 + tid, v);
        }
        return;
    }
    if (b < NWA + NWB) {
        const int col0 = (b - NWA) * 32;
        u32x4 wG[8], wU[8];
#pragma unroll
        for (int i = 0; i < 8; ++i) {
            const size_t off = (size_t)((wave * 8 + i) * 16 + (lane >> 2)) * FF + col0 + 8 * (lane & 3);
            wG[i] = *reinterpret_cast<const u32x4*>(wg + off);
            wU[i] = *reinterpret_cast<const u32x4*>(wu + off);
        }
        const float x0 = bf2f(x[tid]), x1 = bf2f(x[tid + 256]);
        gather<16>(gh + wave * 2 * DM, hp + wave * 2 * DM, lane);
        lds_barrier();
        float h0 = 0.f, h1 = 0.f;
#pragma unroll
        for (int q = 0; q < NRB; ++q) { h0 += hp[q * DM + tid]; h1 += hp[q * DM + tid + 256]; }
        h0 = rbf(x0 + rbf(h0)); h1 = rbf(x1 + rbf(h1));
        {
            float s = block_sum<4>(h0 + h1, red[0], tid);
            const float mean = s * (1.0f / 512.0f);
            const float mbf = rbf(mean);
            const float d0 = h0 - mean, d1 = h1 - mean;
            float ss = block_sum<4>(d0 * d0 + d1 * d1, red[0], tid);
            const float var = rbf(ss * (1.0f / 512.0f));
            const float den = rbf(sqrtf(rbf(var + 1e-5f)));
            ln[tid] = rbf(rbf(h0 - mbf) / den);
            ln[tid + 256] = rbf(rbf(h1 - mbf) / den);
        }
        lds_barrier();
        float acc[16];
#pragma unroll
        for (int i = 0; i < 16; ++i) acc[i] = 0.f;
#pragma unroll
        for (int i = 0; i < 8; ++i) {
            const float lv = ln[(wave * 8 + i) * 16 + (lane >> 2)];
#pragma unroll
            for (int k = 0; k < 4; ++k) {
                acc[2 * k] = fmaf(lv, lo16(wG[i][k]), acc[2 * k]);
                acc[2 * k + 1] = fmaf(lv, hi16(wG[i][k]), acc[2 * k + 1]);
                acc[8 + 2 * k] = fmaf(lv, lo16(wU[i][k]), acc[8 + 2 * k]);
                acc[8 + 2 * k + 1] = fmaf(lv, hi16(wU[i][k]), acc[8 + 2 * k + 1]);
            }
        }
#pragma unroll
        for (int i = 0; i < 16; ++i) {
            float v = acc[i];
            v += dpp<0x124>(v); v += dpp<0x128>(v);
            if ((lane & 15) < 4) hp[((wave * 4 + (lane >> 4)) * 4 + (lane & 3)) * 16 + i] = v;
        }
        lds_barrier();
        if (tid < 32) {
            float g = 0.f, u = 0.f;
#pragma unroll
            for (int q = 0; q < 16; ++q) { g += hp[(q * 4 + (tid >> 3)) * 16 + (tid & 7)]; u += hp[(q * 4 + (tid >> 3)) * 16 + 8 + (tid & 7)]; }
            g = rbf(g); u = rbf(u);
            const float inner = 0.7978845608028654f * (g + 0.044715f * g * g * g);
            const float ge = rbf(0.5f * g * (1.0f + tanhf(inner)));
            publish(gact + col0 + tid, rbf(ge * u));
        }
        return;
    }
    if (b < K2_GRID) {
        const int col0 = (b - NWA - NWB) * 8;
        u32x4 wD[7];
#pragma unroll
        for (int i = 0; i < 7; ++i) wD[i] = *reinterpret_cast<const u32x4*>(wd + (size_t)(tid + 256 * i) * DM + col0);
        const float xv = bf2f(x[col0 + (tid & 7)]);
        if (wave == 0) {
            const u64* g = gh + (lane >> 3) * DM + col0 + (lane & 7);
            u64 v;
            for (int spin = 0; spin < SPIN_CAP; ++spin) {
                v = __hip_atomic_load(const_cast<u64*>(g), RLX_AGENT);
                if (__all((u32)(v >> 32) == TAG)) break;
            }
            float hpart = __builtin_bit_cast(float, (u32)v);
            hpart += dpp<0x128>(hpart);
            hp[lane] = hpart;
        }
        gather<7>(gact + wave * 448, as + wave * 448, lane);
        lds_barrier();
        float acc[8];
#pragma unroll
        for (int i = 0; i < 8; ++i) acc[i] = 0.f;
#pragma unroll
        for (int i = 0; i < 7; ++i) {
            const float av = as[tid + 256 * i];
#pragma unroll
            for (int k = 0; k < 4; ++k) {
                acc[2 * k] = fmaf(av, lo16(wD[i][k]), acc[2 * k]);
                acc[2 * k + 1] = fmaf(av, hi16(wD[i][k]), acc[2 * k + 1]);
            }
        }
#pragma unroll
        for (int i = 0; i < 8; ++i) {
            const float v = wave_sum63(acc[i]);
            if (lane == 63) red[wave][i] = v;
        }
        lds_barrier();
        if (tid < 8) {
            const float v = red[0][tid] + red[1][tid] + red[2][tid] + red[3][tid];
            const float hsum = (hp[tid] + hp[16 + tid]) + (hp[32 + tid] + hp[48 + tid]);
            const float hv = rbf(xv + rbf(hsum));
            out[col0 + tid] = (u16)f2bf_bits(hv + rbf(v));
        }
    }
}

extern "C" void kernel_launch(void* const* d_in, const int* in_sizes, int n_in,
                              void* d_out, int out_size, void* d_ws, size_t ws_size,
                              hipStream_t stream) {
    const u16* x = (const u16*)d_in[0];
    const u16* kc = (const u16*)d_in[1];
    const u16* vc = (const u16*)d_in[2];
    const u16* wq = (const u16*)d_in[3];
    const u16* wo = (const u16*)d_in[4];
    const u16* wg = (const u16*)d_in[6];
    const u16* wu = (const u16*)d_in[7];
    const u16* wd = (const u16*)d_in[8];
    float* ws = (float*)d_ws;
    float* pm = ws;
    float* pl = ws + 256;
    float* po = ws + 512;
    u64* gh = (u64*)(ws + 512 + 256 * 128);
    u64* gact = gh + NRB * DM;
    attn_kernel<<<NWG, K1_THREADS, 0, stream>>>(x, kc, vc, wq, pm, pl, po, gh, NRB * DM + FF);
    tail_kernel<<<K2_GRID, 256, 0, stream>>>(x, wo, wg, wu, wd, pm, pl, po, gh, gact, (u16*)d_out);
}
```
